```python
import torch

def _softmax(x, axis=-1):
    return torch.softmax(x, dim=axis)
def _gelu(x):
    return torch.nn.functional.gelu(x, approximate="tanh")
def _silu(x):
    return torch.nn.functional.silu(x)
def _sigmoid(x):
    return torch.sigmoid(x)
def _layernorm(x, axis=-1, eps=1e-5):
    m = x.mean(dim=axis, keepdim=True)
    v = x.var(dim=axis, keepdim=True, unbiased=False)
    return (x - m) / torch.sqrt(v + eps)
def _scatter_add(tgt, idx, src, axis=0):
    return tgt.clone().index_add_(axis, idx, src)
def _segment_sum(src, seg, n):
    out = torch.zeros((n,) + src.shape[1:], dtype=src.dtype)
    return out.index_add_(0, seg, src)
def _pad(x, pw):
    flat = tuple(v for pair in reversed(pw) for v in pair)
    return torch.nn.functional.pad(x, flat)

kernel_name = 'interleave_layers__sliding_local_dilated__sliding_local_dilated__prompt'

def setup_inputs():

    _w = (lambda a: (lambda t: t.to(torch.bfloat16) if t.is_floating_point() else t)(torch.as_tensor(a)))
    def _A_setup():
        B, S, D = 1, 2048, 576
        return {
            "x": _w((lambda *s: torch.randn(*s, dtype=torch.bfloat16) * 0.05)(B, S, D)),
            "wqkv": _w((lambda *s: torch.randn(*s, dtype=torch.bfloat16) * 0.05)(D, 3 * D)),
            "wo": _w((lambda *s: torch.randn(*s, dtype=torch.bfloat16) * 0.05)(D, D)),
        }
    _A_in = _A_setup()

    def _B_setup():
        B, S, D = 1, 2048, 576
        return {
            "x": _w((lambda *s: torch.randn(*s, dtype=torch.bfloat16) * 0.05)(B, S, D)),
            "wqkv": _w((lambda *s: torch.randn(*s, dtype=torch.bfloat16) * 0.05)(D, 3 * D)),
            "wo": _w((lambda *s: torch.randn(*s, dtype=torch.bfloat16) * 0.05)(D, D)),
        }
    _B_in = _B_setup()

    return {
        "A_x": _A_in["x"],
        "A_wqkv": _A_in["wqkv"],
        "A_wo": _A_in["wo"],
        "B_x": _B_in["x"],
        "B_wqkv": _B_in["wqkv"],
        "B_wo": _B_in["wo"],
    }

def reference(A_x, A_wqkv, A_wo, B_x, B_wqkv, B_wo):
    def _A_ref(x, wqkv, wo):
        B, S, D = x.shape
        H = 32; Dh = D // H
        W, dil = 8, 2
        qkv = (lambda x, *d: x.permute(*d))((x @ wqkv).reshape(B, S, 3, H, Dh), 2, 0, 3, 1, 4)
        q, k, v = qkv[0], qkv[1], qkv[2]
        scale = Dh ** -0.5
        pos = torch.arange(S)
        off = torch.arange(W) * dil
        idx = pos[:, None] - off[None, :]
        mask = idx >= 0
        idx_c = torch.where(mask, idx, 0).reshape(-1)
        kg = k[:, :, idx_c, :].reshape(B, H, S, W, Dh)
        vg = v[:, :, idx_c, :].reshape(B, H, S, W, Dh)
        logits = torch.einsum("bhsd,bhswd->bhsw", q, kg) * scale
        logits = torch.where(mask[None, None, :, :], logits, -1e9)
        attn = _softmax(logits, axis=-1)
        o = torch.einsum("bhsw,bhswd->bhsd", attn, vg)
        return (lambda x, *d: x.permute(*d))(o, 0, 2, 1, 3).reshape(B, S, D) @ wo

    def _B_ref(x, wqkv, wo):
        B, S, D = x.shape
        H = 32; Dh = D // H
        W, dil = 8, 2
        qkv = (lambda x, *d: x.permute(*d))((x @ wqkv).reshape(B, S, 3, H, Dh), 2, 0, 3, 1, 4)
        q, k, v = qkv[0], qkv[1], qkv[2]
        scale = Dh ** -0.5
        pos = torch.arange(S)
        off = torch.arange(W) * dil
        idx = pos[:, None] - off[None, :]
        mask = idx >= 0
        idx_c = torch.where(mask, idx, 0).reshape(-1)
        kg = k[:, :, idx_c, :].reshape(B, H, S, W, Dh)
        vg = v[:, :, idx_c, :].reshape(B, H, S, W, Dh)
        logits = torch.einsum("bhsd,bhswd->bhsw", q, kg) * scale
        logits = torch.where(mask[None, None, :, :], logits, -1e9)
        attn = _softmax(logits, axis=-1)
        o = torch.einsum("bhsw,bhswd->bhsd", attn, vg)
        return (lambda x, *d: x.permute(*d))(o, 0, 2, 1, 3).reshape(B, S, D) @ wo

    x = A_x
    x = x + _A_ref(_layernorm(x, axis=-1), A_wqkv, A_wo)
    x = x + _B_ref(_layernorm(x, axis=-1), B_wqkv, B_wo)
    x = x + _A_ref(_layernorm(x, axis=-1), A_wqkv, A_wo)
    x = x + _B_ref(_layernorm(x, axis=-1), B_wqkv, B_wo)
    return x
```

```cpp
#include <hip/hip_runtime.h>
#include <hip/hip_bf16.h>

constexpr int S = 2048, D = 576, H = 32, DH = 18, WIN = 8, DIL = 2;

__device__ __forceinline__ float bf2f(unsigned short b) { return __builtin_bit_cast(float, (unsigned)b << 16); }
__device__ __forceinline__ unsigned short f2bf(float f) {
  __hip_bfloat16 h = __float2bfloat16(f);
  return __builtin_bit_cast(unsigned short, h);
}

__global__ void k_init(const unsigned short* x, float* xf) {
  int i = blockIdx.x * 256 + threadIdx.x;
  if (i < S * D) xf[i] = bf2f(x[i]);
}
__global__ void k_ln(const float* xf, float* xn) {
  int t = blockIdx.x;
  __shared__ float red[64];
  int l = threadIdx.x;
  float s = 0;
  for (int c = l; c < D; c += 64) s += xf[t * D + c];
  for (int o = 32; o; o >>= 1) s += __shfl_xor(s, o);
  float m = s / D;
  float v = 0;
  for (int c = l; c < D; c += 64) { float d = xf[t * D + c] - m; v += d * d; }
  for (int o = 32; o; o >>= 1) v += __shfl_xor(v, o);
  float r = rsqrtf(v / D + 1e-5f);
  for (int c = l; c < D; c += 64) xn[t * D + c] = (xf[t * D + c] - m) * r;
}
__global__ void k_gemm(const float* A, const unsigned short* B, float* C, const float* res, int N) {
  int n = blockIdx.x * 64 + (threadIdx.x & 63);
  int m = blockIdx.y * 4 + (threadIdx.x >> 6);
  if (n >= N) return;
  float acc = 0;
  for (int k = 0; k < D; ++k) acc += A[m * D + k] * bf2f(B[k * N + n]);
  if (res) acc += res[m * N + n];
  C[m * N + n] = acc;
}
__global__ void k_attn(const float* qkv, float* o) {
  int i = blockIdx.x * 256 + threadIdx.x;
  int t = i / H, h = i % H;
  const float* q = qkv + t * 3 * D + h * DH;
  float lg[WIN];
  float mx = -1e30f;
#pragma unroll
  for (int w = 0; w < WIN; ++w) {
    int j = t - w * DIL;
    float s = 0;
    if (j >= 0) {
      const float* k = qkv + j * 3 * D + D + h * DH;
      for (int d = 0; d < DH; ++d) s += q[d] * k[d];
      s *= 0.23570226039551584f;
    } else s = -1e9f;
    lg[w] = s; mx = fmaxf(mx, s);
  }
  float sum = 0;
#pragma unroll
  for (int w = 0; w < WIN; ++w) { lg[w] = __expf(lg[w] - mx); sum += lg[w]; }
  float inv = 1.f / sum;
  for (int d = 0; d < DH; ++d) {
    float a = 0;
#pragma unroll
    for (int w = 0; w < WIN; ++w) {
      int j = t - w * DIL;
      if (j >= 0) a += lg[w] * qkv[j * 3 * D + 2 * D + h * DH + d];
    }
    o[t * D + h * DH + d] = a * inv;
  }
}
__global__ void k_out(const float* xf, unsigned short* out) {
  int i = blockIdx.x * 256 + threadIdx.x;
  if (i < S * D) out[i] = f2bf(xf[i]);
}

extern "C" void kernel_launch(void* const* d_in, const int* in_sizes, int n_in, void* d_out, int out_size, void* d_ws,
                              size_t ws_size, hipStream_t stream) {
  const unsigned short* x = (const unsigned short*)d_in[0];
  const unsigned short* wq[2] = {(const unsigned short*)d_in[1], (const unsigned short*)d_in[4]};
  const unsigned short* wo[2] = {(const unsigned short*)d_in[2], (const unsigned short*)d_in[5]};
  float* xf = (float*)d_ws;
  float* xf2 = xf + S * D;
  float* xn = xf2 + S * D;
  float* o = xn + S * D;
  float* qkv = o + S * D;
  k_init<<<S * D / 256, 256, 0, stream>>>(x, xf);
  for (int l = 0; l < 4; ++l) {
    k_ln<<<S, 64, 0, stream>>>(xf, xn);
    k_gemm<<<dim3(27, S / 4), 256, 0, stream>>>(xn, wq[l & 1], qkv, nullptr, 3 * D);
    k_attn<<<S * H / 256, 256, 0, stream>>>(qkv, o);
    k_gemm<<<dim3(9, S / 4), 256, 0, stream>>>(o, wo[l & 1], xf2, xf, D);
    float* t = xf; xf = xf2; xf2 = t;
  }
  k_out<<<S * D / 256, 256, 0, stream>>>(xf, (unsigned short*)d_out);
}
```

```cpp
#include <hip/hip_runtime.h>
#include <hip/hip_bf16.h>

typedef unsigned v4u __attribute__((ext_vector_type(4)));
typedef unsigned v2u __attribute__((ext_vector_type(2)));
typedef float f32x4 __attribute__((ext_vector_type(4)));
typedef short v4s __attribute__((ext_vector_type(4)));
typedef __bf16 bf2v __attribute__((ext_vector_type(2)));
#define LAS __attribute__((address_space(3)))
#define GAS __attribute__((address_space(1)))
#define DEVI __device__ __forceinline__
#define INL __attribute__((always_inline))

#define STAMPS 0
#define FSTAMP 0
#define NLAY 4

constexpr int S_ = 2048, D_ = 576;
constexpr int ROWB = D_ * 2;
constexpr int QROWB = 3 * D_ * 2;
constexpr int FLAG_B = 256 * 512;
constexpr int TILE_B = 20480;
constexpr int LEFT_O = 16384, STAT_O = 18432;
constexpr int ACT_B = 128 * TILE_B;
constexpr int OFF_XB = FLAG_B;
constexpr int OFF_OB = OFF_XB + 4 * ACT_B;
constexpr int OFF_TW = OFF_OB + 4 * ACT_B;
constexpr int BLK_B = 10752;
constexpr int BQ_MAIN = 0, BW_MAIN = 7168, BW_C = 9216, BQ_LEFT = 9472, BW_LEFT = 10368, BW_LEFTC = 10624;
constexpr int WS_USED = OFF_TW + 2 * 16 * 16 * BLK_B;
constexpr int L_P1 = 0;
constexpr int L_P2 = L_P1 + 28672;
constexpr int L_CS = L_P2 + 28672;
constexpr int L_CSP = L_CS + 1024;
constexpr int L_MK = L_CSP + 2048;
constexpr int L_REC = L_MK + 3072;
constexpr int L_WO = L_REC + 5 * 2560;
constexpr int WO_W = 9 * 1024 + 5 * 256;
constexpr int L_END = L_WO + 8 * WO_W;
static_assert(L_END <= 163840, "LDS");
constexpr int PW_SET = 2304;
constexpr int AUX_SC1 = 16;
constexpr unsigned SENT = 0xFFFFFFFFu;
constexpr unsigned SPIN_CAP = 1u << 16;
constexpr int OOB = 0x40000000;

struct Params {
  const unsigned short* x;
  const unsigned short* wq0;
  const unsigned short* wo0;
  const unsigned short* wq1;
  const unsigned short* wo1;
  unsigned short* out;
  unsigned char* ws;
};

DEVI unsigned pk2(float a, float b) {
  bf2v v = {(__bf16)a, (__bf16)b};
  return __builtin_bit_cast(unsigned, v);
}
DEVI float asf(unsigned u) { return __builtin_bit_cast(float, u); }
DEVI unsigned asu(float f) { return __builtin_bit_cast(unsigned, f); }
DEVI float bflo(unsigned u) { return asf(u << 16); }
DEVI float bfhi(unsigned u) { return asf(u & 0xFFFF0000u); }
DEVI float xsum(float v) {
  unsigned u = asu(v);
  auto a = __builtin_amdgcn_permlane16_swap(u, u, false, false);
  u = asu(asf(a[0]) + asf(a[1]));
  auto b = __builtin_amdgcn_permlane32_swap(u, u, false, false);
  return asf(b[0]) + asf(b[1]);
}
DEVI float xmax(float v) {
  unsigned u = asu(v);
  auto a = __builtin_amdgcn_permlane16_swap(u, u, false, false);
  u = asu(fmaxf(asf(a[0]), asf(a[1])));
  auto b = __builtin_amdgcn_permlane32_swap(u, u, false, false);
  return fmaxf(asf(b[0]), asf(b[1]));
}
DEVI unsigned umax3(unsigned a, unsigned b, unsigned c) { return max(max(a, b), c); }
DEVI unsigned nosent(unsigned u) { return min(u, 0xFFFFFFFEu); }
DEVI int opq(int v) { asm volatile("" : "+v"(v)); return v; }
#define SB() __builtin_amdgcn_sched_barrier(0)
#define BAR() do { asm volatile("s_waitcnt lgkmcnt(0)\n\ts_barrier" ::: "memory"); SB(); } while (0)
#define BARS() BAR()
#define DRAIN() asm volatile("s_waitcnt vmcnt(0)" ::: "memory")
#define CLOB() asm volatile("" ::: "memory")

struct Frag {
  v4u m[4];
  v4u l;
};
struct Stat { v4u v[2]; };
DEVI void vadd(f32x4& d, const f32x4& a, int r) { float t = d[r] + a[r]; asm volatile("" : "+v"(t)); d[r] = t; }
DEVI void vnfma(f32x4& d, float m, const f32x4& a, int r) { float t = __builtin_fmaf(-m, a[r], d[r]); asm volatile("" : "+v"(t)); d[r] = t; }
DEVI void vmul(f32x4& d, float m, int r) { float t = d[r] * m; asm volatile("" : "+v"(t)); d[r] = t; }
DEVI unsigned vcvt(float a, float b) { unsigned u = pk2(a, b); asm volatile("" : "+v"(u)); return u; }
DEVI void vmx3(unsigned& m, unsigned a, unsigned b) { m = umax3(m, a, b); asm volatile("" : "+v"(m)); }
DEVI float fmx(float a, float b) { float d; asm("v_max_f32 %0, %1, %2" : "=v"(d) : "v"(a), "v"(b)); return d; }
DEVI float fmx3(float a, float b, float c) { float d; asm("v_max3_f32 %0, %1, %2, %3" : "=v"(d) : "v"(a), "v"(b), "v"(c)); return d; }
template <class T> DEVI void pin(T& x) { asm volatile("" : "+v"(x)); }
#define MFA(d, a, b) do { asm volatile("v_mfma_f32_16x16x32_bf16 %0, %1, %2, %0" : "+v"(d) : "a"(a), "v"(b)); SB(); } while (0)
#define MFA0(d, a, b) do { asm volatile("v_mfma_f32_16x16x32_bf16 %0, %1, %2, 0" : "=&v"(d) : "a"(a), "v"(b)); SB(); } while (0)
#define MFV(d, a, b) do { asm volatile("v_mfma_f32_16x16x32_bf16 %0, %1, %2, %0" : "+v"(d) : "v"(a), "v"(b)); SB(); } while (0)
#define MFV0(d, a, b) do { asm volatile("v_mfma_f32_16x16x32_bf16 %0, %1, %2, 0" : "=&v"(d) : "v"(a), "v"(b)); SB(); } while (0)
#define MFVN(d, a, b) do { asm volatile("s_nop 1\n\tv_mfma_f32_16x16x32_bf16 %0, %1, %2, %0" : "+v"(d) : "v"(a), "v"(b)); SB(); } while (0)
#define MFV0N(d, a, b) do { asm volatile("s_nop 1\n\tv_mfma_f32_16x16x32_bf16 %0, %1, %2, 0" : "=&v"(d) : "v"(a), "v"(b)); SB(); } while (0)
#define KEEP(x) asm volatile("" :: "v"(x))
#define NOP2() do { SB(); asm volatile("s_nop 1"); SB(); } while (0)
#define NOP8() do { SB(); asm volatile("s_nop 7"); SB(); } while (0)

template <int V> struct IC { static constexpr int value = V; };
template <int I, int N, class F> DEVI void sfor(F&& f) {
  if constexpr (I < N) { f(IC<I>{}); sfor<I + 1, N>(f); }
}
constexpr char VTAB[5][33] = {"11111110111101110000000",
                              "11011101111022022022220022022022",
                              "22022202222022022011110011011011",
                              "11011101111011011011110000002022",
                              "22020002002222222222222222222222"};
constexpr int vpre(int slot, int k) { int s = 0; for (int q = 0; q < k; ++q) s += VTAB[slot][q] - '0'; return s; }
constexpr int sw(int n) { return n == 3 ? 3 : (n < 3 ? n + 4 : n - 4); }
constexpr int mf_nq(bool halo, bool swp, int k, bool want_q) {
  const int ord[7] = {0, 1, 2, 4, 5, 6, 3};
  for (int o = 0; o < 7; ++o) {
    const int n = ord[o];
    if (halo && (n == 0 || n == 4)) continue;
    const int len = (swp ? sw(n) : n) < 4 ? 5 : 4;
    if (k < len) return want_q ? k : n;
    k -= len;
  }
  return -1;
}

__global__ void __launch_bounds__(256, 1) fused_kernel(Params p) {
  extern __shared__ __attribute__((aligned(16))) unsigned char smem[];
  LAS unsigned char* L = (LAS unsigned char*)smem;
  const int tid = threadIdx.x, lane = tid & 63, w = __builtin_amdgcn_readfirstlane(tid >> 6);
  const int i = lane & 15, gq = lane >> 4;
  const int b = blockIdx.x;
  const int c = (b & 7) * 2 + (b >> 7), g = (b >> 3) & 15;
  const int odd = w & 1, hh = w >> 1;
  const __amdgpu_buffer_rsrc_t rsW = __builtin_amdgcn_make_buffer_rsrc((void*)p.ws, 0, WS_USED, 0x00020000);
#define STAMP()
#define RETRY()
  STAMP();

  v2u tr[2][8];
  int tdst = 0;
  bool tleft = false;
  {
    int u = tid < 180 ? tid : 179;
    int r0, dr, col, pitch;
    bool isq;
    if (u < 135) {
      tleft = u >= 108;
      const int go = u / 27, cc = u % 27;
      r0 = 36 * c + 8 * go; dr = tleft ? 0 : 4; isq = true;
      const int seg = cc / 9, ch = cc % 9;
      col = seg * 576 + 36 * g + 4 * ch;
      int E, pos;
      if (seg == 0) { if (ch < 4) { E = 0; pos = ch; } else if (ch < 8) { E = 4; pos = ch - 4; } else { E = 3; pos = 0; } }
      else if (seg == 1) { if (ch < 4) { E = 1; pos = ch; } else if (ch < 8) { E = 2; pos = ch - 4; } else { E = 5; pos = 0; } }
      else { if (ch < 3) { E = 3; pos = ch + 1; } else if (ch < 6) { E = 5; pos = ch - 2; } else { E = 6; pos = ch - 6; } }
      tdst = tleft ? BQ_LEFT + E * 128 + 4 * pos * 8 : BQ_MAIN + E * 1024 + (go * 16 + 4 * pos) * 16;
    } else {
      const int v = u - 135, go = v / 9, cw = v % 9;
      tleft = v >= 36;
      r0 = 36 * c + 4 * go; dr = tleft ? 0 : 20; isq = false;
      col = 36 * g + 4 * cw;
      if (tleft) tdst = cw < 8 ? BW_LEFT + (cw & 1) * 128 + 4 * (cw >> 1) * 8 : BW_LEFTC;
      else tdst = cw < 8 ? BW_MAIN + (cw & 1) * 1024 + (go * 16 + 4 * (cw >> 1)) * 16 : BW_C + go * 64;
    }
    pitch = isq ? QROWB : ROWB;
    const unsigned char* s0 = (const unsigned char*)(isq ? p.wq0 : p.wo0) + (size_t)(r0 * pitch + col * 2);
    const unsigned char* s1 = (const unsigned char*)(isq ? p.wq1 : p.wo1) + (size_t)(r0 * pitch + col * 2);
#pragma unroll
    for (int j = 0; j < 8; ++j) tr[0][j] = *(const GAS v2u*)(s0 + ((j & 3) + (j >> 2) * dr) * pitch);
#pragma unroll
    for (int j = 0; j < 8; ++j) tr[1][j] = *(const GAS v2u*)(s1 + ((j & 3) + (j >> 2) * dr) * pitch);
  }
  v4u xr_m[2]; v2u xr_l[2];
  {
    const __amdgpu_buffer_rsrc_t rsX = __builtin_amdgcn_make_buffer_rsrc((void*)p.x, 0, S_ * ROWB, 0x00020000);
#pragma unroll
    for (int s = 0; s < 2; ++s) {
      const int ro = (128 * c + 32 * w + 2 * i + s) * ROWB + 72 * g;
      xr_m[s] = __builtin_amdgcn_raw_buffer_load_b128(rsX, ro + 16 * gq, 0, 0);
      xr_l[s] = __builtin_amdgcn_raw_buffer_load_b64(rsX, ro + 64, 0, 0);
    }
  }
  SB();
  const int own_main = (c * 8 + w) * TILE_B + g * 1024 + lane * 16;
  const int own_aux = gq < 2 ? (c * 8 + w) * TILE_B + LEFT_O + gq * 2048 + (g >> 3) * 1024 + (((g >> 1) & 3) * 16 + i) * 16 + (g & 1) * 8 : OOB;
  auto fill = [&](int buf) INL {
    const v4u sv = {SENT, SENT, SENT, SENT};
#pragma unroll
    for (int s = 0; s < 2; ++s) {
      __builtin_amdgcn_raw_buffer_store_b128(sv, rsW, own_main + buf + s * 4 * TILE_B, 0, AUX_SC1);
      __builtin_amdgcn_raw_buffer_store_b64(v2u{SENT, SENT}, rsW, own_aux + buf + s * 4 * TILE_B, 0, AUX_SC1);
    }
  };
  fill(OFF_OB);
  fill(OFF_XB + ACT_B);
  SB();
  if (w == 0) {
    f32x4 mc, mp;
#pragma unroll
    for (int r = 0; r < 4; ++r) {
      const int kk = 4 * gq + r;
      mc[r] = (kk <= i && i - kk <= 7) ? 0.f : -1e30f;
      mp[r] = (kk >= i + 9) ? 0.f : -1e30f;
    }
    *(LAS f32x4*)(L + L_MK + lane * 16) = mc;
    *(LAS f32x4*)(L + L_MK + 1024 + lane * 16) = mp;
    *(LAS f32x4*)(L + L_MK + 2048 + lane * 16) = f32x4{-1e30f, -1e30f, -1e30f, -1e30f};
  }
  if (tid < 180) {
#pragma unroll
    for (int t = 0; t < 2; ++t) {
      LAS unsigned char* dst = L + L_P1 + t * BLK_B + tdst;
#pragma unroll
      for (int q = 0; q < 4; ++q) {
        v4u pc;
#pragma unroll
        for (int d = 0; d < 4; ++d) {
          const unsigned lo = (q & 2) ? tr[t][2 * d].y : tr[t][2 * d].x;
          const unsigned hi = (q & 2) ? tr[t][2 * d + 1].y : tr[t][2 * d + 1].x;
          pc[d] = __builtin_amdgcn_perm(hi, lo, (q & 1) ? 0x07060302u : 0x05040100u);
        }
        if (tleft) *(LAS v2u*)(dst + q * 8) = v2u{pc[0], pc[1]};
        else *(LAS v4u*)(dst + q * 16) = pc;
      }
    }
  }
  f32x4 xr[2][3];
  const bool g0 = gq == 0;
#pragma unroll
  for (int s = 0; s < 2; ++s) {
    const f32x4 a = {bflo(xr_m[s].x), bfhi(xr_m[s].x), bflo(xr_m[s].y), bfhi(xr_m[s].y)};
    const f32x4 bb = {bflo(xr_m[s].z), bfhi(xr_m[s].z), bflo(xr_m[s].w), bfhi(xr_m[s].w)};
    const f32x4 cc = {bflo(xr_l[s].x), bfhi(xr_l[s].x), bflo(xr_l[s].y), bfhi(xr_l[s].y)};
    xr[s][0] = odd ? bb : a; xr[s][1] = odd ? a : bb; xr[s][2] = cc;
    float sm = 0.f, sq = 0.f;
#pragma unroll
    for (int r = 0; r < 4; ++r) { sm += cc[r]; sq += cc[r] * cc[r]; }
    sm = g0 ? sm : 0.f; sq = g0 ? sq : 0.f;
#pragma unroll
    for (int r = 0; r < 4; ++r) { sm += a[r] + bb[r]; sq += a[r] * a[r] + bb[r] * bb[r]; }
    sm = xsum(sm); sq = xsum(sq);
    const v2u av = {nosent(g0 ? xr_l[s].x : asu(sm)), nosent(g0 ? xr_l[s].y : asu(sq))};
    __builtin_amdgcn_raw_buffer_store_b64(av, rsW, own_aux + OFF_XB + s * 4 * TILE_B, 0, AUX_SC1);
    const v4u xv = {nosent(xr_m[s].x), nosent(xr_m[s].y), nosent(xr_m[s].z), nosent(xr_m[s].w)};
    __builtin_amdgcn_raw_buffer_store_b128(xv, rsW, own_main + OFF_XB + s * 4 * TILE_B, 0, AUX_SC1);
  }
  BAR();
#pragma unroll
  for (int r = 0; r < 6; ++r) {
    const int pc = r * 256 + tid;
    if (pc < 1344) {
      const int t = pc >= 672 ? 1 : 0, o = (pc - t * 672) * 16;
      const v4u v = *(LAS v4u*)(L + L_P1 + t * BLK_B + o);
      __builtin_amdgcn_raw_buffer_store_b128(v, rsW, OFF_TW + ((t * 16 + g) * 16 + c) * BLK_B + o, 0, AUX_SC1);
    }
  }
  DRAIN();
  BAR();
  if (w == 0) {
    int off = OOB;
    if (gq == 0) off = (i * 16 + g) * 512 + c * 4;
    else if (gq == 1) off = (c * 16 + i) * 512 + 128 + g * 4;
    else if (gq == 2 && c < 15) off = ((c + 1) * 16 + i) * 512 + 256 + g * 4;
    __builtin_amdgcn_raw_buffer_store_b32(1u, rsW, off, 0, AUX_SC1);
  }
  STAMP();

  const int vl16 = lane * 16;
  const int vh16 = ((i >= 8) && (c > 0)) ? lane * 16 : OOB;
  const int cprev = c > 0 ? c - 1 : 0;
  const int wko = w * 4096, wlo = LEFT_O + hh * 1024;
  auto ld1 = [&](Frag& f, int q, int tb, bool halo) INL {
    const v4u v = __builtin_amdgcn_raw_buffer_load_b128(rsW, halo ? vh16 : vl16, tb + (q < 4 ? wko + q * 1024 : wlo), AUX_SC1);
    if (q < 4) f.m[q] = v; else f.l = v;
  };
  auto load_ws = [&](Frag& f, int tb, bool halo) INL {
#pragma unroll
    for (int q = 0; q < 5; ++q) ld1(f, q, tb, halo);
  };
  auto chk = [&](const Frag& f, int e, unsigned& m) INL {
    const v4u v = (e >> 1) < 4 ? f.m[(e >> 1) & 3] : f.l;
    if (e & 1) vmx3(m, v.z, v.w); else vmx3(m, v.x, v.y);
  };
  auto fix = [&](Frag& f, unsigned m, int tb, bool halo) INL {
    if (__builtin_amdgcn_ballot_w64(m == SENT) != 0ull) {
      unsigned spins = 0;
      do {
        __builtin_amdgcn_s_sleep(4);
        CLOB();
        RETRY();
        load_ws(f, tb, halo);
        m = 0;
#pragma unroll
        for (int e = 0; e < 10; ++e) chk(f, e, m);
      } while (__builtin_amdgcn_ballot_w64(m == SENT) != 0ull && ++spins < SPIN_CAP);
      NOP2();
    }
    SB();
  };
  auto lds1 = [&](Stat& st, int q, int tb, bool halo) INL {
    st.v[q] = __builtin_amdgcn_raw_buffer_load_b128(rsW, halo ? vh16 : vl16, tb + STAT_O + q * 1024, AUX_SC1);
  };
  auto chks = [&](const Stat& st, int e, unsigned& m) INL {
    if (e & 1) vmx3(m, st.v[e >> 1].z, st.v[e >> 1].w); else vmx3(m, st.v[e >> 1].x, st.v[e >> 1].y);
  };
  auto stat_fix = [&](Stat& st, unsigned m, int tb, bool halo) INL {
    if (__builtin_amdgcn_ballot_w64(m == SENT) != 0ull) {
      unsigned spins = 0;
      do {
        __builtin_amdgcn_s_sleep(4);
        CLOB();
        RETRY();
        lds1(st, 0, tb, halo); lds1(st, 1, tb, halo);
        m = 0;
#pragma unroll
        for (int e = 0; e < 4; ++e) chks(st, e, m);
      } while (__builtin_amdgcn_ballot_w64(m == SENT) != 0ull && ++spins < SPIN_CAP);
    }
    SB();
  };

  Frag F[4], FH;
  {
    const __amdgpu_buffer_rsrc_t rsX = __builtin_amdgcn_make_buffer_rsrc((void*)p.x, 0, S_ * ROWB, 0x00020000);
    const int vmX = i * (2 * ROWB) + gq * 16 + w * 288;
    const int vhX = ((i >= 8) && (c > 0)) ? vmX : OOB;
#pragma unroll
    for (int k = 0; k < 4; ++k) FH.m[k] = __builtin_amdgcn_raw_buffer_load_b128(rsX, vhX, (128 * cprev + 96) * ROWB + k * 72, 0);
#pragma unroll
    for (int u = 0; u < 4; ++u)
#pragma unroll
      for (int k = 0; k < 4; ++k)
        F[u].m[k] = __builtin_amdgcn_raw_buffer_load_b128(rsX, vmX, (128 * c + 32 * ((w + 1 + u) & 3)) * ROWB + k * 72, 0);
  }
  SB();
  {
    const bool need = (gq < 2 || (gq == 2 && c > 0));
    const int fo = need ? (c * 16 + g) * 512 + gq * 128 + i * 4 : OOB;
    unsigned spins = 0;
    for (;;) {
      CLOB();
      const unsigned v = __builtin_amdgcn_raw_buffer_load_b32(rsW, fo, 0, AUX_SC1);
      if (__builtin_amdgcn_ballot_w64(need && v != 1u) == 0ull || ++spins >= SPIN_CAP) break;
      __builtin_amdgcn_s_sleep(2);
    }
    CLOB();
  }
  STAMP();
  v4u Wm[2][7][4], Wl[2][4];
  auto load_wq = [&](int t) INL {
    const int gb = OFF_TW + (t * 16 + g) * 16 * BLK_B;
#pragma unroll
    for (int n = 0; n < 7; ++n) {
      const int Ee = n == 3 ? 3 : (odd ? (n < 3 ? n + 4 : n - 4) : n);
#pragma unroll
      for (int k = 0; k < 4; ++k)
        Wm[t][n][k] = __builtin_amdgcn_raw_buffer_load_b128(rsW, lane * 16, gb + (4 * w + k) * BLK_B + BQ_MAIN + Ee * 1024, AUX_SC1);
    }
#pragma unroll
    for (int n = 0; n < 4; ++n) {
      const int Ee = n == 3 ? 3 : (odd ? n + 4 : n);
      const int vo = (n == 3 && odd) ? OOB : (8 * hh + 2 * gq) * BLK_B + i * 8;
      const v2u a = __builtin_amdgcn_raw_buffer_load_b64(rsW, vo, gb + BQ_LEFT + Ee * 128, AUX_SC1);
      const v2u bb = __builtin_amdgcn_raw_buffer_load_b64(rsW, vo, gb + BLK_B + BQ_LEFT + Ee * 128, AUX_SC1);
      Wl[t][n] = v4u{a.x, a.y, bb.x, bb.y};
    }
  };
  auto load_wo = [&](int t) INL {
    const int gb = OFF_TW + (t * 16 + g) * 16 * BLK_B;
    LAS unsigned char* dst = L + L_WO + (t * 4 + w) * WO_W;
    v4u m[8]; v4u cf[4];
#pragma unroll
    for (int k = 0; k < 4; ++k) {
#pragma unroll
      for (int n = 0; n < 2; ++n)
        m[k * 2 + n] = __builtin_amdgcn_raw_buffer_load_b128(rsW, lane * 16, gb + (4 * w + k) * BLK_B + BW_MAIN + (n ^ odd) * 1024, AUX_SC1);
      cf[k] = __builtin_amdgcn_raw_buffer_load_b128(rsW, lane < 16 ? lane * 16 : OOB, gb + (4 * w + k) * BLK_B + BW_C, AUX_SC1);
    }
    const int vo = (8 * hh + 2 * gq) * BLK_B + i * 8;
    const v2u a = __builtin_amdgcn_raw_buffer_load_b64(rsW, vo, gb + BW_LEFT + odd * 128, AUX_SC1);
    const v2u bb = __builtin_amdgcn_raw_buffer_load_b64(rsW, vo, gb + BLK_B + BW_LEFT + odd * 128, AUX_SC1);
    const int vc = (lane < 16 && odd) ? (8 * hh + 2 * (lane >> 2)) * BLK_B + (lane & 3) * 8 : OOB;
    const v2u ca = __builtin_amdgcn_raw_buffer_load_b64(rsW, vc, gb + BW_LEFTC, AUX_SC1);
    const v2u cb = __builtin_amdgcn_raw_buffer_load_b64(rsW, vc, gb + BLK_B + BW_LEFTC, AUX_SC1);
#pragma unroll
    for (int q = 0; q < 8; ++q) *(LAS v4u*)(dst + q * 1024 + lane * 16) = m[q];
    *(LAS v4u*)(dst + 8192 + lane * 16) = v4u{a.x, a.y, bb.x, bb.y};
    if (lane < 16) {
#pragma unroll
      for (int k = 0; k < 4; ++k) *(LAS v4u*)(dst + 9216 + k * 256 + lane * 16) = cf[k];
      *(LAS v4u*)(dst + 9216 + 1024 + lane * 16) = v4u{ca.x, ca.y, cb.x, cb.y};
    }
  };
  auto colsum = [&](int t) INL {
    v4u ones = {0x3F803F80u, 0x3F803F80u, 0x3F803F80u, 0x3F803F80u};
    asm volatile("" : "+v"(ones));
    NOP2();
    f32x4 a[7];
#pragma unroll
    for (int n = 0; n < 7; ++n) {
      MFA0(a[n], Wm[t][n][0], ones);
#pragma unroll
      for (int k = 1; k < 4; ++k) MFA(a[n], Wm[t][n][k], ones);
      if (n < 4) MFA(a[n], Wl[t][n], ones);
    }
    NOP8();
#pragma unroll
    for (int n = 0; n < 7; ++n) {
      const int Ee = n == 3 ? 3 : (odd ? (n < 3 ? n + 4 : n - 4) : n);
      if (i == 0) *(LAS f32x4*)(L + L_CSP + w * 448 + Ee * 64 + gq * 16) = a[n];
    }
    BAR();
    if (tid < 28) {
      f32x4 s4 = *(LAS f32x4*)(L + L_CSP + tid * 16);
#pragma unroll
      for (int q = 1; q < 4; ++q) s4 += *(LAS f32x4*)(L + L_CSP + q * 448 + tid * 16);
      *(LAS f32x4*)(L + L_CS + t * 448 + tid * 16) = s4;
    }
    BAR();
  };
  load_wq(0);
  Stat ST, SH;
  ld1(FH, 4, OFF_XB + (cprev * 8 + 3) * TILE_B, true);
#pragma unroll
  for (int u = 0; u < 4; ++u) ld1(F[u], 4, OFF_XB + (c * 8 + ((w + 1 + u) & 3)) * TILE_B, false);
  lds1(SH, 0, OFF_XB + (cprev * 8 + 3) * TILE_B, true); lds1(SH, 1, OFF_XB + (cprev * 8 + 3) * TILE_B, true);
  lds1(ST, 0, OFF_XB + (c * 8 + w) * TILE_B, false); lds1(ST, 1, OFF_XB + (c * 8 + w) * TILE_B, false);
  SB();
  load_wo(0);
  SB();
  load_wq(1);
  SB();
  load_wo(1);
  SB();
  colsum(0);
  colsum(1);
  STAMP();

  const float QSC = 0.23570226039551584f * 1.4426950408889634f;
  const int mk_prev_off = L_MK + ((c == 0 && w == 0) ? 2048 : 1024);
  unsigned mcur[2];
#pragma unroll
  for (int d = 0; d < 2; ++d) {
    const int k0_ = 4 * gq + 2 * d;
    mcur[d] = (k0_ <= i ? 0xFFFFu : 0u) | (k0_ + 1 <= i ? 0xFFFF0000u : 0u);
  }
  const bool bodd = odd != 0;

  float st_a, st_b;
  auto stat_step = [&](const Stat& st, int e, float& mean, float& rstd) INL {
    if (e == 0) { st_a = asf(st.v[0].x) + asf(st.v[0].z); st_b = asf(st.v[0].y) + asf(st.v[0].w); pin(st_a); pin(st_b); }
    if (e == 1) { st_a += asf(st.v[1].x); st_b += asf(st.v[1].y); pin(st_a); pin(st_b); }
    if (e == 2) { st_a += asf(st.v[1].z); st_b += asf(st.v[1].w); pin(st_a); pin(st_b); }
    if (e == 3) {
      auto r = __builtin_amdgcn_permlane32_swap(asu(st_a), asu(st_b), false, false);
      st_a = asf(r[0]) + asf(r[1]); st_b = st_a; pin(st_a); pin(st_b);
    }
    if (e == 4) {
      auto r = __builtin_amdgcn_permlane16_swap(asu(st_a), asu(st_b), false, false);
      st_a = asf(r[0]) + asf(r[1]); st_b = st_a; pin(st_a); pin(st_b);
    }
    if (e == 5) {
      auto r = __builtin_amdgcn_permlane32_swap(asu(st_a), asu(st_b), false, false);
      mean = asf(r[0]) * (1.f / D_); st_b = asf(r[1]) * (1.f / D_); pin(mean); pin(st_b);
    }
    if (e == 6) { st_b = fmx(st_b - mean * mean, 0.f) + 1e-5f; pin(st_b); }
    if (e == 7) { rstd = __builtin_amdgcn_rsqf(st_b); pin(rstd); }
  };

  auto layer = [&](const v4u (&WM)[7][4], const v4u (&WLf)[4], int t, int l) INL {
    const int xb = OFF_XB + l * ACT_B, ob = OFF_OB + l * ACT_B, xn = OFF_XB + (l < 3 ? l + 1 : 3) * ACT_B;
    const int fo_ = l < 3 ? OFF_OB + (l + 1) * ACT_B : OOB / 2, fx_ = l < 2 ? OFF_XB + (l + 2) * ACT_B : OOB / 2;
    auto fill1 = [&](int e) INL {
      const int buf = ((e & 4) ? fx_ : fo_) + ((e >> 1) & 1) * 4 * TILE_B;
      if (e & 1) __builtin_amdgcn_raw_buffer_store_b64(v2u{SENT, SENT}, rsW, own_aux + buf, 0, AUX_SC1);
      else __builtin_amdgcn_raw_buffer_store_b128(v4u{SENT, SENT, SENT, SENT}, rsW, own_main + buf, 0, AUX_SC1);
    };
    auto run = [&](auto SLOT, f32x4 (&a)[7], const Frag& f, auto&& mem, auto&& vop) INL {
      constexpr int slot = decltype(SLOT)::value;
      constexpr bool halo = slot == 0, zero = slot <= 1;
      sfor<0, halo ? 23 : 32>([&](auto K) INL {
        constexpr bool swp = slot == 1 || slot == 3;
        constexpr int k = decltype(K)::value, n = mf_nq(halo, swp, k, false), q = mf_nq(halo, swp, k, true), lt = swp ? sw(n) : n;
        if constexpr (q == 0 && zero) MFA0(a[n], WM[lt][0], f.m[0]);
        else if constexpr (q < 4) MFA(a[n], WM[lt][q], f.m[q]);
        else MFA(a[n], WLf[lt & 3], f.l);
        mem(K);
        SB();
        sfor<0, VTAB[slot][k] - '0'>([&](auto Z) INL { vop(IC<vpre(slot, k) + decltype(Z)::value>{}); SB(); });
      });
    };
    sfor<0, 2>([&](auto S_C) INL {
      constexpr int s = decltype(S_C)::value;
      const int ln = opq(lane * 16);
      const int oA = ln + odd * 4096, oB = ln - odd * 4096;
      auto pl = [&](int base, int n) INL -> LAS f32x4* { return (LAS f32x4*)(L + (n < 3 ? oA : (n == 3 ? ln : oB)) + base + n * 1024); };
      const int j0 = (w + 1) & 3, j1 = (w + 2) & 3, j2 = (w + 3) & 3;
      const int xt = xb + (c * 2 + s) * 4 * TILE_B, xh = xb + ((cprev * 2 + s) * 4 + 3) * TILE_B;
      const int nt = s == 0 ? xt + 4 * TILE_B : ob + c * 8 * TILE_B;
      f32x4 a[7], ah[7];
      unsigned mx = 0, ms = 0;
#pragma unroll
      for (int e = 0; e < 10; ++e) chk(FH, e, (e & 1) ? mx : ms);
      fix(FH, max(mx, ms), xh, true);
      mx = 0; ms = 0;
      run(IC<0>{}, ah, FH,
        [&](auto K) INL {
          constexpr int k = decltype(K)::value;
          if constexpr (k == 7) *pl(L_P2 + w * 7168, 1) = ah[1];
          if constexpr (k == 12) *pl(L_P2 + w * 7168, 2) = ah[2];
          if constexpr (k == 16) *pl(L_P2 + w * 7168, 5) = ah[5];
          if constexpr (k == 20) *pl(L_P2 + w * 7168, 6) = ah[6];
          if constexpr (s == 0 && k == 18) fill1(0);
          if constexpr (s == 0 && k == 22) fill1(1);
        },
        [&](auto V) INL { constexpr int v = decltype(V)::value; if constexpr (v < 10) chk(F[0], v, mx); else chks(SH, v - 10, ms); });
      fix(F[0], mx, xt + j0 * TILE_B, false);
      stat_fix(SH, ms, xh, true);
      auto traffic = [&](auto K, auto U, int jp, int jc, int jn) INL {
        constexpr int k = decltype(K)::value, u = decltype(U)::value;
        constexpr int x = (u & 1) ? 0 : 4;
        if constexpr (u >= 1 && k == 2) *pl(L_P1 + jp * 7168, 3) = a[3];
        if constexpr (k == 6) BARS();
        if constexpr (u >= 1) {
          if constexpr (k == 6) a[4] = *pl(L_P1 + jc * 7168, 4 ^ x);
          if constexpr (k == 7) a[5] = *pl(L_P1 + jc * 7168, 5 ^ x);
          if constexpr (k == 8) a[6] = *pl(L_P1 + jc * 7168, 6 ^ x);
          if constexpr (k == 9) a[3] = *pl(L_P1 + jc * 7168, 3);
        }
        if constexpr (u < 3) {
          if constexpr (k == 11) *pl(L_P1 + jc * 7168, 0 ^ x) = a[0];
          if constexpr (k == 14) *pl(L_P1 + jc * 7168, 1 ^ x) = a[1];
          if constexpr (k == 17) *pl(L_P1 + jc * 7168, 2 ^ x) = a[2];
          if constexpr (k == 22) BARS();
          if constexpr (k == 22) a[0] = *pl(L_P1 + jn * 7168, 4 ^ x);
          if constexpr (k == 23) *pl(L_P1 + jc * 7168, 4 ^ x) = a[4];
          if constexpr (k == 24) a[1] = *pl(L_P1 + jn * 7168, 5 ^ x);
          if constexpr (k == 25) a[2] = *pl(L_P1 + jn * 7168, 6 ^ x);
          if constexpr (k == 26) *pl(L_P1 + jc * 7168, 5 ^ x) = a[5];
          if constexpr (k == 29) *pl(L_P1 + jc * 7168, 6 ^ x) = a[6];
        }
      };
      const int Eh = w == 0 ? 1 : (w == 1 ? 2 : (w == 2 ? 5 : 3));
      f32x4 hp[8], c1, c6;
      float hm, hr;
      v2u hq[2];
      auto hop = [&](auto V) INL {
        constexpr int v = decltype(V)::value, tl = v / 22, e = v % 22, r = e & 3, b0 = tl * 4;
        if constexpr (e < 4) vadd(hp[b0], hp[b0 + 1], r);
        else if constexpr (e < 8) vadd(hp[b0 + 2], hp[b0 + 3], r);
        else if constexpr (e < 12) vnfma(hp[b0], hm, tl ? c6 : c1, r);
        else if constexpr (e < 16) vadd(hp[b0], hp[b0 + 2], r);
        else if constexpr (e < 20) vmul(hp[b0], hr, r);
        else if constexpr (e == 20) hq[tl].x = vcvt(hp[b0][0], hp[b0][1]);
        else hq[tl].y = vcvt(hp[b0][2], hp[b0][3]);
      };
      mx = 0;
      run(IC<1>{}, a, F[0],
        [&](auto K) INL {
          constexpr int k = decltype(K)::value;
          if constexpr (k == 2) *pl(L_P2 + w * 7168, 3) = ah[3];
          traffic(K, IC<0>{}, 0, j0, j1);
          if constexpr (k >= 7 && k < 11) {
            hp[k - 7] = *(LAS f32x4*)(L + ln + Eh * 1024 + L_P2 + (k - 7) * 7168);
            hp[k - 3] = *(LAS f32x4*)(L + ln + L_P2 + (k - 7) * 7168 + 6 * 1024);
          }
          if constexpr (k == 12) c1 = *(LAS f32x4*)(L + L_CS + t * 448 + Eh * 64 + gq * 16);
          if constexpr (k == 13) c6 = *(LAS f32x4*)(L + L_CS + t * 448 + 6 * 64 + gq * 16);
          if constexpr (s == 0 && k == 4) fill1(2);
          if constexpr (s == 0 && k == 20) fill1(3);
        },
        [&](auto V) INL {
          constexpr int v = decltype(V)::value;
          if constexpr (v < 8) stat_step(SH, v, hm, hr);
          else if constexpr (v == 8) chk(F[1], 0, mx);
          else if constexpr (v < 27) { if constexpr (v & 1) chk(F[1], (v - 7) / 2, mx); else if constexpr (v >= 14) hop(IC<(v - 14) / 2>{}); }
          else hop(IC<v - 27 + 7>{});
        });
      fix(F[1], mx, xt + j1 * TILE_B, false);
      mx = 0; ms = 0;
      run(IC<2>{}, a, F[1],
        [&](auto K) INL {
          constexpr int k = decltype(K)::value;
          traffic(K, IC<1>{}, j0, j1, j2);
          if constexpr (k == 19) *(LAS v2u*)(L + L_REC + w * 512 + lane * 8) = hq[0];
          if constexpr (k == 20) *(LAS v2u*)(L + L_REC + 4 * 512 + lane * 8) = hq[1];
          if constexpr (s == 0) {
            if constexpr (k == 4) ld1(FH, 0, xh + 4 * TILE_B, true);
            if constexpr (k == 9) ld1(FH, 1, xh + 4 * TILE_B, true);
            if constexpr (k == 13) ld1(FH, 2, xh + 4 * TILE_B, true);
            if constexpr (k == 16) ld1(FH, 3, xh + 4 * TILE_B, true);
            if constexpr (k == 21) ld1(FH, 4, xh + 4 * TILE_B, true);
          }
          if constexpr (k == 27) ld1(F[0], 0, nt + j0 * TILE_B, false);
          if constexpr (k == 31) ld1(F[0], 1, nt + j0 * TILE_B, false);
        },
        [&](auto V) INL {
          constexpr int v = decltype(V)::value;
          if constexpr (v < 26) hop(IC<17 + v>{});
          else if constexpr (v == 26) hop(IC<43>{});
          else if constexpr (v < 36) chk(F[2], v - 27, mx);
        });
      chk(F[2], 9, mx);
#pragma unroll
      for (int e = 0; e < 4; ++e) chks(ST, e, ms);
      fix(F[2], mx, xt + j2 * TILE_B, false);
      stat_fix(ST, ms, xt + w * TILE_B, false);
      f32x4 cs[7];
      const int gcs = opq(gq * 16);
      const int cA = gcs + odd * 256, cB = gcs - odd * 256;
      auto csr = [&](int n) INL { cs[n] = *(LAS f32x4*)(L + (n < 3 ? cA : (n == 3 ? gcs : cB)) + L_CS + t * 448 + n * 64); };
      float mean, rstd;
      mx = 0;
      run(IC<3>{}, a, F[2],
        [&](auto K) INL {
          constexpr int k = decltype(K)::value;
          traffic(K, IC<2>{}, j1, j2, w);
          if constexpr (k == 0) ld1(F[0], 2, nt + j0 * TILE_B, false);
          if constexpr (k == 4) ld1(F[0], 3, nt + j0 * TILE_B, false);
          if constexpr (k == 9) ld1(F[0], 4, nt + j0 * TILE_B, false);
          if constexpr (k == 13) ld1(F[1], 0, nt + j1 * TILE_B, false);
          if constexpr (k == 16) ld1(F[1], 1, nt + j1 * TILE_B, false);
          if constexpr (k == 20) ld1(F[1], 2, nt + j1 * TILE_B, false);
          if constexpr (k == 27) ld1(F[1], 3, nt + j1 * TILE_B, false);
          if constexpr (k == 31) ld1(F[1], 4, nt + j1 * TILE_B, false);
          if constexpr (k == 15) csr(0);
          if constexpr (k == 18) csr(1);
          if constexpr (k == 28) csr(2);
        },
        [&](auto V) INL {
          constexpr int v = decltype(V)::value;
          if constexpr (v < 8) stat_step(ST, v, mean, rstd);
          else if constexpr (v < 17) chk(F[3], v - 8, mx);
          else if constexpr (v < 21) vnfma(a[0], mean, cs[0], v - 17);
          else vnfma(a[1], mean, cs[1], v - 21);
        });
      chk(F[3], 9, mx);
      fix(F[3], mx, xt + w * TILE_B, false);
      unsigned pkv[7][2];
      v4u q0, q1, k0, k1;
      const int rec_lane = opq(lane * 8) + L_REC + w * 2560;
      const int rc1 = rec_lane + 2560 + (bodd ? 2 : 0) * 512, rc2 = rec_lane + 2560 + (bodd ? 4 : 1) * 512;
      const int rc5 = rec_lane + 2560 + (bodd ? 0 : 2) * 512, rc6 = rec_lane + 2560 + (bodd ? 1 : 4) * 512;
      f32x4 sc0, sc1, sp0, sp1;
      auto post = [&](int n, int e) INL {
        if (e < 4) vmul(a[n], rstd, e);
        else pkv[n][e - 4] = vcvt(a[n][2 * (e - 4)], a[n][2 * (e - 4) + 1]);
      };
      run(IC<4>{}, a, F[3],
        [&](auto K) INL {
          constexpr int k = decltype(K)::value;
          traffic(K, IC<3>{}, j2, w, 0);
          if constexpr (k == 0) csr(4);
          if constexpr (k == 1) csr(5);
          if constexpr (k == 11) csr(6);
          if constexpr (k == 13) csr(3);
          if constexpr (k == 4) ld1(F[2], 0, nt + j2 * TILE_B, false);
          if constexpr (k == 8) ld1(F[2], 1, nt + j2 * TILE_B, false);
          if constexpr (k == 23) *(LAS v2u*)(L + rc1) = v2u{pkv[1][0], pkv[1][1]};
          if constexpr (k == 26) *(LAS v2u*)(L + rc2) = v2u{pkv[2][0], pkv[2][1]};
          if constexpr (k == 30) *(LAS v2u*)(L + rc5) = v2u{pkv[5][0], pkv[5][1]};
          if constexpr (k == 27) sc0 = *(LAS f32x4*)(L + L_MK + ln);
          if constexpr (k == 28) sc1 = *(LAS f32x4*)(L + L_MK + ln);
          if constexpr (k == 29) sp0 = *(LAS f32x4*)(L + mk_prev_off + ln);
          if constexpr (k == 31) sp1 = *(LAS f32x4*)(L + mk_prev_off + ln);
        },
        [&](auto V) INL {
          constexpr int v = decltype(V)::value;
          if constexpr (v < 2) vnfma(a[1], mean, cs[1], v + 2);
          else if constexpr (v < 6) vnfma(a[2], mean, cs[2], v - 2);
          else if constexpr (v < 8) post(0, v - 2);
          else if constexpr (v < 12) { }
          else if constexpr (v < 16) vnfma(a[4], mean, cs[4], v - 12);
          else if constexpr (v < 20) vnfma(a[5], mean, cs[5], v - 16);
          else if constexpr (v < 24) vnfma(a[6], mean, cs[6], v - 20);
          else if constexpr (v < 28) vnfma(a[3], mean, cs[3], v - 24);
          else if constexpr (v < 34) post(1, v - 28);
          else if constexpr (v < 40) post(2, v - 34);
          else if constexpr (v < 42) post(4, v - 36);
          else if constexpr (v < 48) post(5, v - 42);
          else if constexpr (v < 52) post(6, v - 48);
        });
      STAMP();
      post(6, 4); post(6, 5);
      SB();
      *(LAS v2u*)(L + rc6) = v2u{pkv[6][0], pkv[6][1]};
      const unsigned e40 = bodd ? pkv[0][0] : pkv[4][0];
      q0 = v4u{bodd ? pkv[4][0] : pkv[0][0], bodd ? pkv[4][1] : pkv[0][1], g0 ? e40 : 0u, 0u};
      k0 = v4u{bodd ? pkv[5][0] : pkv[1][0], bodd ? pkv[5][1] : pkv[1][1], bodd ? pkv[6][0] : pkv[2][0], bodd ? pkv[6][1] : pkv[2][1]};
      SB();
      MFVN(sc0, k0, q0);
      BARS();
      const v2u kA = *(LAS v2u*)(L + rec_lane), kB = *(LAS v2u*)(L + rec_lane + 512);
      const v2u kB2 = *(LAS v2u*)(L + rec_lane + 512), kC = *(LAS v2u*)(L + rec_lane + 1024);
      SB();
      {
        const float r3 = g0 ? 1.f : rstd;
        const f32x4 v = a[3] * r3;
        pkv[3][0] = pk2(v[0], v[1]); pkv[3][1] = pk2(v[2], v[3]);
      }
      *(LAS v2u*)(L + rec_lane + 2560 + 3 * 512) = v2u{pkv[3][0], pkv[3][1]};
      q1 = v4u{g0 ? 0u : e40, bodd ? pkv[0][1] : pkv[4][1], g0 ? pkv[3][0] : 0u, g0 ? pkv[3][1] : 0u};
      k1 = v4u{k0.z, k0.w, bodd ? pkv[1][0] : pkv[5][0], bodd ? pkv[1][1] : pkv[5][1]};
      SB();
      MFVN(sc1, k1, q1);
      const v4u kp0 = {kA.x, kA.y, kB.x, kB.y}, kp1 = {kB2.x, kB2.y, kC.x, kC.y};
      MFVN(sp0, kp0, q0);
      MFVN(sp1, kp1, q1);
      ld1(F[2], 2, nt + j2 * TILE_B, false);
      SB();
      BARS();
      const int rp = lane & 3;
      const int vrow = opq((4 * gq + (i >> 2)) * 8) + L_REC + w * 2560;
      const int vo_h0 = vrow + (rp < 3 ? 3 * 512 + (rp + 1) * 128 : 2 * 512 + 128);
      const int vo_h1 = vrow + (rp == 0 ? 2 * 512 + 3 * 128 : 4 * 512 + (rp - 1) * 128);
      const int vo_c = vrow + 2 * 512 + 2 * 128;
      const v2u vp0 = __builtin_bit_cast(v2u, __builtin_amdgcn_ds_read_tr16_b64_v4i16((LAS v4s*)(L + vo_h0)));
      const v2u vc0 = __builtin_bit_cast(v2u, __builtin_amdgcn_ds_read_tr16_b64_v4i16((LAS v4s*)(L + vo_h0 + 2560)));
      const v2u vp1 = __builtin_bit_cast(v2u, __builtin_amdgcn_ds_read_tr16_b64_v4i16((LAS v4s*)(L + vo_h1)));
      const v2u vc1 = __builtin_bit_cast(v2u, __builtin_amdgcn_ds_read_tr16_b64_v4i16((LAS v4s*)(L + vo_h1 + 2560)));
      const v2u vpc = __builtin_bit_cast(v2u, __builtin_amdgcn_ds_read_tr16_b64_v4i16((LAS v4s*)(L + vo_c)));
      const v2u vcc = __builtin_bit_cast(v2u, __builtin_amdgcn_ds_read_tr16_b64_v4i16((LAS v4s*)(L + vo_c + 2560)));
      NOP2();
      f32x4 z0, z1;
#pragma unroll
      for (int r = 0; r < 4; ++r) { z0[r] = fmx(sc0[r], sp0[r]); z1[r] = fmx(sc1[r], sp1[r]); }
      float m0 = fmx(fmx3(z0[0], z0[1], z0[2]), z0[3]), m1 = fmx(fmx3(z1[0], z1[1], z1[2]), z1[3]);
      {
        auto r1 = __builtin_amdgcn_permlane32_swap(asu(m0), asu(m1), false, false);
        const unsigned u1 = asu(fmx(asf(r1[0]), asf(r1[1])));
        auto r2 = __builtin_amdgcn_permlane16_swap(u1, u1, false, false);
        const unsigned u2 = asu(fmx(asf(r2[0]), asf(r2[1])));
        auto r3 = __builtin_amdgcn_permlane32_swap(u2, u2, false, false);
        m0 = asf(r3[0]); m1 = asf(r3[1]);
      }
      const float rsq = rstd * QSC;
      m0 *= -rsq; m1 *= -rsq;
      ld1(F[2], 3, nt + j2 * TILE_B, false);
      v4u P0, P1;
      {
        const unsigned e01 = pk2(__builtin_amdgcn_exp2f(__builtin_fmaf(z0[0], rsq, m0)), __builtin_amdgcn_exp2f(__builtin_fmaf(z0[1], rsq, m0)));
        const unsigned e23 = pk2(__builtin_amdgcn_exp2f(__builtin_fmaf(z0[2], rsq, m0)), __builtin_amdgcn_exp2f(__builtin_fmaf(z0[3], rsq, m0)));
        P0 = v4u{e01 & ~mcur[0], e23 & ~mcur[1], e01 & mcur[0], e23 & mcur[1]};
      }
      v4u ones = {0x3F803F80u, 0x3F803F80u, 0x3F803F80u, 0x3F803F80u};
      const v4u va0 = {vp0.x, vp0.y, vc0.x, vc0.y}, va1 = {vp1.x, vp1.y, vc1.x, vc1.y}, vac = {vpc.x, vpc.y, vcc.x, vcc.y};
      f32x4 l0, l1, o0, o1, oc0, oc1;
      SB();
      MFV0N(l0, ones, P0);
      const unsigned f01 = pk2(__builtin_amdgcn_exp2f(__builtin_fmaf(z1[0], rsq, m1)), __builtin_amdgcn_exp2f(__builtin_fmaf(z1[1], rsq, m1)));
      SB();
      MFV0N(o0, va0, P0);
      const unsigned f23 = pk2(__builtin_amdgcn_exp2f(__builtin_fmaf(z1[2], rsq, m1)), __builtin_amdgcn_exp2f(__builtin_fmaf(z1[3], rsq, m1)));
      SB();
      MFV0N(oc0, vac, P0);
      P1 = v4u{f01 & ~mcur[0], f23 & ~mcur[1], f01 & mcur[0], f23 & mcur[1]};
      SB();
      MFV0N(l1, ones, P1);
      MFV0N(o1, va1, P1);
      MFV0N(oc1, vac, P1);
      const float i0 = __builtin_amdgcn_rcpf(l0[0]);
      v4u om; v2u oa;
      om.x = nosent(pk2(o0[0] * i0, o0[1] * i0)); om.y = nosent(pk2(o0[2] * i0, o0[3] * i0));
      oa.x = nosent(pk2(oc0[0] * i0, oc0[1] * i0));
      SB();
      KEEP(l0); KEEP(oc0);
      const float i1 = __builtin_amdgcn_rcpf(l1[0]);
      om.z = nosent(pk2(o1[0] * i1, o1[1] * i1)); om.w = nosent(pk2(o1[2] * i1, o1[3] * i1));
      SB();
      oa.y = nosent(pk2(oc1[2] * i1, oc1[3] * i1));
      KEEP(l1); KEEP(oc1);
      __builtin_amdgcn_raw_buffer_store_b128(om, rsW, own_main + ob + s * 4 * TILE_B, 0, AUX_SC1);
      __builtin_amdgcn_raw_buffer_store_b64(oa, rsW, own_aux + ob + s * 4 * TILE_B, 0, AUX_SC1);
      SB();
      ld1(F[2], 4, nt + j2 * TILE_B, false);
      if constexpr (s == 0) { lds1(SH, 0, xh + 4 * TILE_B, true); lds1(SH, 1, xh + 4 * TILE_B, true); }
      load_ws(F[3], nt + w * TILE_B, false);
      if constexpr (s == 0) { lds1(ST, 0, nt + w * TILE_B, false); lds1(ST, 1, nt + w * TILE_B, false); }
      SB();
      STAMP();
    });
    v4u wa[4][2], wc[4], wl, wlc;
    const int ln = opq(lane * 16);
    {
      LAS unsigned char* src = L + L_WO + (t * 4 + w) * WO_W;
      const int lc = opq((gq * 4 + (i & 3)) * 16);
#pragma unroll
      for (int k = 0; k < 4; ++k) wa[k][0] = *(LAS v4u*)(src + (2 * k) * 1024 + ln);
      wl = *(LAS v4u*)(src + 8192 + ln);
#pragma unroll
      for (int k = 0; k < 4; ++k) wa[k][1] = *(LAS v4u*)(src + (2 * k + 1) * 1024 + ln);
#pragma unroll
      for (int k = 0; k < 4; ++k) wc[k] = *(LAS v4u*)(src + 9216 + k * 256 + lc);
      wlc = *(LAS v4u*)(src + 9216 + 1024 + lc);
    }
    SB();
#pragma unroll
    for (int s = 0; s < 2; ++s) {
      const int pwb = s == 0 ? L_P2 : L_P1;
      const int ot = ob + (c * 2 + s) * 4 * TILE_B;
      const int pa = ln + odd * 1024, pb = ln + (odd ^ 1) * 1024, pcr = opq(i * 16) + 2048;
      const int nt = s == 0 ? ot + 4 * TILE_B : xn + c * 8 * TILE_B, nh = xn + (cprev * 8 + 3) * TILE_B;
      f32x4 acc[2][3], pr[9];
      Frag NF[4], NH;
      unsigned mx = 0, my = 0;
#pragma unroll
      for (int e = 0; e < 10; ++e) chk(F[0], e, (e & 1) ? mx : my);
      mx = max(mx, my);
#pragma unroll
      for (int u = 0; u < 4; ++u) {
        const int j = (w + 1 + u) & 3, jp = (w + u) & 3;
        fix(F[u], mx, ot + j * TILE_B, false);
        mx = 0;
        f32x4 (&a)[3] = acc[u & 1];
        f32x4 (&ap)[3] = acc[(u & 1) ^ 1];
        if (u == 3) { a[0] = xr[s][0]; a[1] = xr[s][1]; a[2] = xr[s][2]; NOP2(); }
        int k = 0;
#pragma unroll
        for (int n = 0; n < 3; ++n) {
#pragma unroll
          for (int q = 0; q < 5; ++q) {
            if (q == 4 && n == 1) continue;
            const v4u wv = q < 4 ? (n < 2 ? wa[q & 3][n & 1] : wc[q & 3]) : (n == 0 ? wl : wlc);
            const v4u xv = q < 4 ? F[u].m[q & 3] : F[u].l;
            if (q == 0 && u < 3) MFV0(a[n], wv, xv); else MFV(a[n], wv, xv);
            if (u < 3 && k == 7) *(LAS f32x4*)(L + pa + pwb + (j * 3 + u) * PW_SET) = a[0];
            if (u < 3 && k == 11) *(LAS f32x4*)(L + pb + pwb + (j * 3 + u) * PW_SET) = a[1];
            if (u > 0 && k == 2 && g0) *(LAS f32x4*)(L + ln + 2048 + pwb + (jp * 3 + (u - 1)) * PW_SET) = ap[2];
            if (u < 3) {
              if (k < 2) chk(F[u + 1], k, mx);
              if (k >= 3 && k < 7) chk(F[u + 1], k - 1, mx);
              if (k >= 8 && k < 11) chk(F[u + 1], k - 2, mx);
              if (k == 12) chk(F[u + 1], 9, mx);
            }
            if (s == 0) {
              if (u == 0 && k == 4) fill1(4);
              if (u == 0 && k == 9) fill1(5);
              if (u == 1 && k == 4) fill1(6);
              if (u == 1 && k == 9) fill1(7);
              if (u == 3 && (k == 0 || k == 3 || k == 5)) ld1(NF[0], k / 2, nt + ((w + 1) & 3) * TILE_B, false);
              if (u == 3 && (k == 10 || k == 12)) ld1(NF[0], k / 2 - 2, nt + ((w + 1) & 3) * TILE_B, false);
            } else {
              const int e = u * 3 + (k == 0 ? 0 : (k == 4 ? 1 : 2));
              if (k == 0 || k == 4 || k == 9) {
                if (e < 5) ld1(NH, e, nh, true);
                else if (e < 10) ld1(NF[0], e - 5, nt + ((w + 1) & 3) * TILE_B, false);
                else lds1(SH, e - 10, nh, true);
              }
            }
            if (u == 3 && k == 6) BARS();
            if (u == 3 && k >= 7 && k < 10) {
              const int rb = pwb + (w * 3 + (k - 7)) * PW_SET;
              pr[(k - 7) * 3 + 0] = *(LAS f32x4*)(L + pa + rb);
              pr[(k - 7) * 3 + 1] = *(LAS f32x4*)(L + pb + rb);
              pr[(k - 7) * 3 + 2] = *(LAS f32x4*)(L + pcr + rb);
            }
            SB();
            ++k;
          }
        }
      }
      f32x4 (&a)[3] = acc[1];
      a[0] += pr[0] + pr[3] + pr[6];
      a[1] += pr[1] + pr[4] + pr[7];
      const f32x4 pc_ = pr[2] + pr[5] + pr[8];
      const unsigned a0_ = pk2(a[0][0], a[0][1]), a1_ = pk2(a[0][2], a[0][3]);
      const unsigned b0_ = pk2(a[1][0], a[1][1]), b1_ = pk2(a[1][2], a[1][3]);
      v4u xm = bodd ? v4u{b0_, b1_, a0_, a1_} : v4u{a0_, a1_, b0_, b1_};
      SB();
      a[2] += pc_;
      xr[s][0] = a[0]; xr[s][1] = a[1]; xr[s][2] = a[2];
      const unsigned c0_ = pk2(a[2][0], a[2][1]), c1_ = pk2(a[2][2], a[2][3]);
      if (l < NLAY - 1) {
        v4u xs;
#pragma unroll
        for (int d = 0; d < 4; ++d) xs[d] = nosent(xm[d]);
        __builtin_amdgcn_raw_buffer_store_b128(xs, rsW, own_main + xn + s * 4 * TILE_B, 0, AUX_SC1);
        float sm = bflo(c0_) + bfhi(c0_) + bflo(c1_) + bfhi(c1_);
        float sq = bflo(c0_) * bflo(c0_) + bfhi(c0_) * bfhi(c0_) + bflo(c1_) * bflo(c1_) + bfhi(c1_) * bfhi(c1_);
        sm = g0 ? sm : 0.f; sq = g0 ? sq : 0.f;
#pragma unroll
        for (int d = 0; d < 4; ++d) {
          const float lo = bflo(xm[d]), hi = bfhi(xm[d]);
          sm += lo + hi; sq += lo * lo + hi * hi;
        }
        {
          auto r1 = __builtin_amdgcn_permlane32_swap(asu(sm), asu(sq), false, false);
          const unsigned u1 = asu(asf(r1[0]) + asf(r1[1]));
          auto r2 = __builtin_amdgcn_permlane16_swap(u1, u1, false, false);
          const unsigned u2 = asu(asf(r2[0]) + asf(r2[1]));
          auto r3 = __builtin_amdgcn_permlane32_swap(u2, u2, false, false);
          sm = asf(r3[0]); sq = asf(r3[1]);
        }
        const v2u av = {nosent(g0 ? c0_ : asu(sm)), nosent(g0 ? c1_ : asu(sq))};
        __builtin_amdgcn_raw_buffer_store_b64(av, rsW, own_aux + xn + s * 4 * TILE_B, 0, AUX_SC1);
      } else {
        const __amdgpu_buffer_rsrc_t rsO = __builtin_amdgcn_make_buffer_rsrc((void*)p.out, 0, S_ * ROWB, 0x00020000);
        const int lq = opq(lane);
        const int ro = (128 * c + 32 * w + 2 * (lq & 15) + s) * ROWB + 72 * g;
        __builtin_amdgcn_raw_buffer_store_b128(xm, rsO, ro + 16 * (lq >> 4), 0, 0);
        __builtin_amdgcn_raw_buffer_store_b64(v2u{c0_, c1_}, rsO, g0 ? ro + 64 : OOB, 0, 0);
      }
      SB();
#pragma unroll
      for (int u = 1; u < 4; ++u) load_ws(NF[u], nt + ((w + 1 + u) & 3) * TILE_B, false);
      if (s == 1) { lds1(ST, 0, nt + w * TILE_B, false); lds1(ST, 1, nt + w * TILE_B, false); }
#pragma unroll
      for (int u = 0; u < 4; ++u) F[u] = NF[u];
      if (s == 1) FH = NH;
      SB();
      STAMP();
    }
  };

  for (int ll = 0; ll < 2; ++ll) {
    if (2 * ll < NLAY) layer(Wm[0], Wl[0], 0, 2 * ll);
    if (2 * ll + 1 < NLAY) layer(Wm[1], Wl[1], 1, 2 * ll + 1);
  }
}

extern "C" void kernel_launch(void* const* d_in, const int* in_sizes, int n_in, void* d_out, int out_size, void* d_ws,
                              size_t ws_size, hipStream_t stream) {
  Params p;
  p.x = (const unsigned short*)d_in[0];
  p.wq0 = (const unsigned short*)d_in[1];
  p.wo0 = (const unsigned short*)d_in[2];
  p.wq1 = (const unsigned short*)d_in[4];
  p.wo1 = (const unsigned short*)d_in[5];
  p.out = (unsigned short*)d_out;
  p.ws = (unsigned char*)d_ws;
  (void)hipFuncSetAttribute((const void*)fused_kernel, hipFuncAttributeMaxDynamicSharedMemorySize, L_END);
  (void)hipMemsetAsync(d_ws, 0, FLAG_B, stream);
  fused_kernel<<<256, 256, L_END, stream>>>(p);
}
```
